```python
import torch

def _softmax(x, axis=-1):
    return torch.softmax(x, dim=axis)
def _gelu(x):
    return torch.nn.functional.gelu(x, approximate="tanh")
def _silu(x):
    return torch.nn.functional.silu(x)
def _sigmoid(x):
    return torch.sigmoid(x)
def _layernorm(x, axis=-1, eps=1e-5):
    m = x.mean(dim=axis, keepdim=True)
    v = x.var(dim=axis, keepdim=True, unbiased=False)
    return (x - m) / torch.sqrt(v + eps)
def _scatter_add(tgt, idx, src, axis=0):
    return tgt.clone().index_add_(axis, idx, src)
def _segment_sum(src, seg, n):
    out = torch.zeros((n,) + src.shape[1:], dtype=src.dtype)
    return out.index_add_(0, seg, src)
def _pad(x, pw):
    flat = tuple(v for pair in reversed(pw) for v in pair)
    return torch.nn.functional.pad(x, flat)

kernel_name = 'parallel_sum__gqa_block_cached__geglu_ffn__decode'

def setup_inputs():

    _w = (lambda a: (lambda t: t.to(torch.bfloat16) if t.is_floating_point() else t)(torch.as_tensor(a)))
    def _A_setup():
        B, Sq, D = 2, 1, 640
        Hq, Hkv, Sk, Dh = 32, 32, 4096, 128
        assert Hq % Hkv == 0, (Hq, Hkv)
        return {
            "x": _w((lambda *s: torch.randn(*s, dtype=torch.bfloat16) * 0.05)(B, Sq, D)),
            "k_cache": _w((lambda *s: torch.randn(*s, dtype=torch.bfloat16) * 3.2)(B, Hkv, Sk, Dh)),
            "v_cache": _w((lambda *s: torch.randn(*s, dtype=torch.bfloat16) * 1.6)(B, Hkv, Sk, Dh)),
            "wq": _w((lambda *s: torch.randn(*s, dtype=torch.bfloat16) * 3.2)(D, Hq * Dh) * (D ** -0.5)),
            "wo": _w((lambda *s: torch.randn(*s, dtype=torch.bfloat16) * 1.6)(Hq * Dh, D) * ((Hq * Dh) ** -0.5)),
        }
    _A_in = _A_setup()

    def _B_setup():
        B, S, D, F = 2, 1, 640, 1728
        return {
            "x": _w((lambda *s: torch.randn(*s, dtype=torch.bfloat16) * 0.05)(B, S, D)),
            "w_gate": _w((lambda *s: torch.randn(*s, dtype=torch.bfloat16) * 0.05)(D, F)),
            "w_up": _w((lambda *s: torch.randn(*s, dtype=torch.bfloat16) * 0.05)(D, F)),
            "w_down": _w((lambda *s: torch.randn(*s, dtype=torch.bfloat16) * 0.05)(F, D)),
        }
    _B_in = _B_setup()

    return {
        "A_x": _A_in["x"],
        "A_k_cache": _A_in["k_cache"],
        "A_v_cache": _A_in["v_cache"],
        "A_wq": _A_in["wq"],
        "A_wo": _A_in["wo"],
        "B_x": _B_in["x"],
        "B_w_gate": _B_in["w_gate"],
        "B_w_up": _B_in["w_up"],
        "B_w_down": _B_in["w_down"],
    }

def reference(A_x, A_k_cache, A_v_cache, A_wq, A_wo, B_x, B_w_gate, B_w_up, B_w_down):
    def _A_ref(x, k_cache, v_cache, wq, wo):
        B, Sq, D = x.shape
        Hq, Dh = 32, 128
        Hkv = k_cache.shape[1]
        rep = Hq // Hkv
        q = (lambda x, *d: x.permute(*d))((x @ wq).reshape(B, Sq, Hq, Dh), 0, 2, 1, 3)
        k = (lambda x, n, axis: torch.repeat_interleave(x, n, dim=axis))(k_cache, rep, axis=1)
        v = (lambda x, n, axis: torch.repeat_interleave(x, n, dim=axis))(v_cache, rep, axis=1)
        scores = torch.einsum("bhqd,bhkd->bhqk", q, k) * (Dh ** -0.5)
        p = _softmax(scores, axis=-1)
        o = torch.einsum("bhqk,bhkd->bhqd", p, v)
        return (lambda x, *d: x.permute(*d))(o, 0, 2, 1, 3).reshape(B, Sq, Hq * Dh) @ wo

    def _B_ref(x, w_gate, w_up, w_down):
        return (_gelu(x @ w_gate) * (x @ w_up)) @ w_down

    x = A_x
    xn = _layernorm(x, axis=-1)
    return x + _A_ref(xn, A_k_cache, A_v_cache, A_wq, A_wo) + _B_ref(xn, B_w_gate, B_w_up, B_w_down)
```

```cpp
#define APOLL 3
#define PDELAY 75
#define JT 22
#define EW 1
#define WG2 44
#define WPRE2 3
#define TG 928
#define WPRE 1
#define DV 32
#define QDELAY 480
#define OFF0 2200
#define FLATE 0
#define OUT_AUX 16
#define P2DELAY 30
#define KH 13
#define G2C 2
#define EPO 0
#define WGATE 20
#define CODE_BYTES 19056
#include <hip/hip_runtime.h>
#include <stdint.h>

#define KV_AUX 2
#define W2_AUX 2
#define NS 6
#define LAG 3
#define CATCH 4
#define XS 0
#define FDELAY 800
#define HDELAY 1300
#define WGATE2 WGATE
#define WTOUCH 0
#define WTS 16384
#define WPREF 8
#define RADJ 0
#define WHOLD 0
#define WHW 3
#define GAP 0
#define CONV 20
#define JV 16
#define GFD 0
#define GFS 0
#define GQS 0
#define HMEM 7
#define WPOST 8
#define WDIAG 0
#define DIAG 0

#define ENDWAIT "s_waitcnt vmcnt(0)"
typedef unsigned u32;
typedef unsigned long long u64;
typedef u32 u32x4 __attribute__((ext_vector_type(4)));
typedef u32 u32x2 __attribute__((ext_vector_type(2)));
typedef float f32x4 __attribute__((ext_vector_type(4)));
typedef float f32x2 __attribute__((ext_vector_type(2)));
typedef __bf16 bf16x8 __attribute__((ext_vector_type(8)));
typedef __bf16 bf16x2 __attribute__((ext_vector_type(2)));
typedef __attribute__((address_space(1))) u32x4 gu32x4;

constexpr u32 WS_ST = 4096;
constexpr u32 WS_GQ = 16384;
constexpr u32 WS_GF = WS_GQ + 256 * 1024;
constexpr u32 WS_G1 = WS_GF + 256 * 1024;
constexpr u32 WS_G2 = WS_G1 + 256 * 640;
constexpr u32 WS_GH = WS_G2 + 240 * 2048;
constexpr u32 WS_END = WS_GH + 2 * 864 * 8;
constexpr u32 WS_DIAG = 8u << 20;
constexpr u32 TAG_STEP = 0x9E3779B9u;
constexpr u32 SPIN_CAP = 400000u;
constexpr u32 OOB = 0x80000000u;
constexpr float L2E = 1.4426950408889634f;
constexpr int SC1 = 16;

#define SB() __builtin_amdgcn_sched_barrier(0)
#define MEMCLOB() asm volatile("" ::: "memory")

__device__ __forceinline__ u32 pk_bf16(float a, float b) {
  bf16x2 v = __builtin_convertvector((f32x2){a, b}, bf16x2);
  return __builtin_bit_cast(u32, v);
}
__device__ __forceinline__ float bf_lo(u32 w) { return __uint_as_float(w << 16); }
__device__ __forceinline__ float bf_hi(u32 w) { return __uint_as_float(w & 0xffff0000u); }
__device__ __forceinline__ float rbf(float a) { return bf_lo(pk_bf16(a, a)); }
__device__ __forceinline__ float dot2(u32 a, u32 b, float c) {
  return __builtin_amdgcn_fdot2_f32_bf16(__builtin_bit_cast(bf16x2, a), __builtin_bit_cast(bf16x2, b), c, false);
}
template <int CTRL> __device__ __forceinline__ u32 dppu(u32 v) { return __builtin_amdgcn_update_dpp(0u, v, CTRL, 0xf, 0xf, true); }
template <int CTRL> __device__ __forceinline__ float dppf(float v) { return __uint_as_float(dppu<CTRL>(__float_as_uint(v))); }
template <int CTRL, int BANK> __device__ __forceinline__ u32 dppm(u32 old, u32 v) { return __builtin_amdgcn_update_dpp(old, v, CTRL, 0xf, BANK, false); }
template <int BANK> __device__ __forceinline__ float mergef(float a, float b) {
  return __uint_as_float(dppm<0xE4, BANK>(__float_as_uint(a), __float_as_uint(b)));
}
constexpr int QP_X1 = 0xB1, QP_X2 = 0x4E, ROW_ROR4 = 0x124, ROW_ROR8 = 0x128, HALF_MIRROR = 0x141, NEWBCAST = 0x150;
__device__ __forceinline__ float xsum16(float v) { auto r = __builtin_amdgcn_permlane16_swap(__float_as_uint(v), __float_as_uint(v), false, false); return __uint_as_float(r[0]) + __uint_as_float(r[1]); }
__device__ __forceinline__ float xsum32(float v) { auto r = __builtin_amdgcn_permlane32_swap(__float_as_uint(v), __float_as_uint(v), false, false); return __uint_as_float(r[0]) + __uint_as_float(r[1]); }
__device__ __forceinline__ float xmax16(float v) { auto r = __builtin_amdgcn_permlane16_swap(__float_as_uint(v), __float_as_uint(v), false, false); return fmaxf(__uint_as_float(r[0]), __uint_as_float(r[1])); }
__device__ __forceinline__ float xmax32(float v) { auto r = __builtin_amdgcn_permlane32_swap(__float_as_uint(v), __float_as_uint(v), false, false); return fmaxf(__uint_as_float(r[0]), __uint_as_float(r[1])); }
__device__ __forceinline__ float ex2(float v) { return __builtin_amdgcn_exp2f(v); }
__device__ __forceinline__ f32x4 gemv_step(u32 dup, const u32x4& mask, const u32x4& w, f32x4 acc) {
  u32x4 a = {dup & mask.x, dup & mask.y, dup & mask.z, dup & mask.w};
  return __builtin_amdgcn_mfma_f32_16x16x32_bf16(__builtin_bit_cast(bf16x8, a), __builtin_bit_cast(bf16x8, w), acc, 0, 0, 0);
}
__device__ __forceinline__ void wg_barrier() {
  asm volatile("s_waitcnt lgkmcnt(0)\n\ts_barrier" ::: "memory");
}
#define STAMP(i) do {} while (0)
#define STAMP_DECL
#define STAMP_DUMP() do {} while (0)

template <int I> struct IC { static constexpr int v = I; };
template <int A, int B, class F> __device__ __forceinline__ void sfor(F&& f) {
  if constexpr (A < B) { f(IC<A>{}); sfor<A + 1, B>(f); }
}
#define INL __attribute__((always_inline))
constexpr int cto(int j) {
  int c = 0;
  for (int i = 2 * NS; i <= j; ++i) { int t = c + CATCH < i - LAG ? c + CATCH : i - LAG; if (i < JT && t > KH) t = KH; c = t > i - 2 * NS + 1 ? t : i - 2 * NS + 1; }
  return c;
}
__device__ __forceinline__ u32 now() { return (u32)__builtin_amdgcn_s_memrealtime(); }
__device__ __forceinline__ void wait_until(u32 due) {
  SB();
  asm volatile("s_branch 2f\n1:\n\ts_sleep 1\n2:\n\ts_memrealtime vcc\n\ts_waitcnt lgkmcnt(0)\n\ts_sub_i32 vcc_lo, vcc_lo, %0\n\t"
               "s_cmp_lt_i32 vcc_lo, 0\n\ts_cbranch_scc1 1b" :: "s"(due) : "vcc", "scc", "memory");
  SB();
}
#define POLL1_ASM \
  "buffer_load_dwordx4 v[128:131], %[va], %[rs], 0 offen sc1\n" \
  "buffer_load_dwordx4 v[132:135], %[va], %[rs], 0 offen offset:640 sc1\n" \
  "buffer_load_dwordx4 v[136:139], %[va], %[rs], 0 offen offset:1280 sc1\n" \
  "buffer_load_dwordx4 v[140:143], %[va], %[rs], 0 offen offset:1920 sc1\n" \
  "buffer_load_dwordx4 v[144:147], %[vm], %[rs], 0 offen sc1\n" \
  "1:\n" \
  "s_waitcnt vmcnt(4)\n" \
  "v_cmpx_eq_u32_e32 vcc, %[tag], v129\n" \
  "v_cmpx_eq_u32_e32 vcc, %[tag], v131\n" \
  "s_waitcnt vmcnt(3)\n" \
  "v_cmpx_eq_u32_e32 vcc, %[tag], v133\n" \
  "v_cmpx_eq_u32_e32 vcc, %[tag], v135\n" \
  "s_waitcnt vmcnt(2)\n" \
  "v_cmpx_eq_u32_e32 vcc, %[tag], v137\n" \
  "v_cmpx_eq_u32_e32 vcc, %[tag], v139\n" \
  "s_waitcnt vmcnt(1)\n" \
  "v_cmpx_eq_u32_e32 vcc, %[tag], v141\n" \
  "v_cmpx_eq_u32_e32 vcc, %[tag], v143\n" \
  "s_waitcnt vmcnt(0)\n" \
  "v_cmpx_eq_u32_e32 vcc, %[tag], v145\n" \
  "v_cmpx_eq_u32_e32 vcc, %[tag], v147\n" \
  "s_cmp_eq_u64 vcc, -1\n" \
  "s_mov_b64 exec, -1\n" \
  "s_cbranch_scc1 2f\n" \
  "buffer_load_dwordx4 v[128:131], %[va], %[rs], 0 offen sc1\n" \
  "buffer_load_dwordx4 v[132:135], %[va], %[rs], 0 offen offset:640 sc1\n" \
  "buffer_load_dwordx4 v[136:139], %[va], %[rs], 0 offen offset:1280 sc1\n" \
  "buffer_load_dwordx4 v[140:143], %[va], %[rs], 0 offen offset:1920 sc1\n" \
  "buffer_load_dwordx4 v[144:147], %[vm], %[rs], 0 offen sc1\n" \
  "s_add_u32 %[cnt], %[cnt], 1\n" \
  "s_cmp_lt_u32 %[cnt], 0x100000\n" \
  "s_cbranch_scc1 1b\n" \
  "s_waitcnt vmcnt(0)\n" \
  "2:\n" \
  "v_mov_b32 %[x0], v128\n" \
  "v_mov_b32 %[z0], v130\n" \
  "v_mov_b32 %[x1], v132\n" \
  "v_mov_b32 %[z1], v134\n" \
  "v_mov_b32 %[x2], v136\n" \
  "v_mov_b32 %[z2], v138\n" \
  "v_mov_b32 %[x3], v140\n" \
  "v_mov_b32 %[z3], v142\n" \
  "v_mov_b32 %[x4], v144\n" \
  "v_mov_b32 %[z4], v146\n" \
  "9:\n" \
  "s_nop 1\n"

#define POLL2_ASM \
  "buffer_load_dwordx4 v[96:99], %[vc], %[rs], 0 offen sc1\n" \
  "buffer_load_dwordx4 v[100:103], %[va], %[rs], 0 offen sc1\n" \
  "buffer_load_dwordx4 v[104:107], %[vb], %[rs], 0 offen sc1\n" \
  "s_sleep 6\n" \
  "buffer_load_dwordx4 v[108:111], %[vc], %[rs], 0 offen sc1\n" \
  "buffer_load_dwordx4 v[112:115], %[va], %[rs], 0 offen sc1\n" \
  "buffer_load_dwordx4 v[116:119], %[vb], %[rs], 0 offen sc1\n" \
  "1:\n" \
  "s_waitcnt vmcnt(5)\n" \
  "v_cmpx_eq_u32_e32 vcc, %[tag], v97\n" \
  "v_cmpx_eq_u32_e32 vcc, %[tag], v99\n" \
  "s_waitcnt vmcnt(4)\n" \
  "v_cmpx_eq_u32_e32 vcc, %[tag], v101\n" \
  "v_cmpx_eq_u32_e32 vcc, %[tag], v103\n" \
  "s_waitcnt vmcnt(3)\n" \
  "v_cmpx_eq_u32_e32 vcc, %[tag], v105\n" \
  "v_cmpx_eq_u32_e32 vcc, %[tag], v107\n" \
  "s_cmp_eq_u64 vcc, -1\n" \
  "s_mov_b64 exec, -1\n" \
  "s_cbranch_scc1 2f\n" \
  "buffer_load_dwordx4 v[96:99], %[vc], %[rs], 0 offen sc1\n" \
  "buffer_load_dwordx4 v[100:103], %[va], %[rs], 0 offen sc1\n" \
  "buffer_load_dwordx4 v[104:107], %[vb], %[rs], 0 offen sc1\n" \
  "s_add_u32 %[cnt], %[cnt], 1\n" \
  "s_sleep 5\n" \
  "s_waitcnt vmcnt(5)\n" \
  "v_cmpx_eq_u32_e32 vcc, %[tag], v109\n" \
  "v_cmpx_eq_u32_e32 vcc, %[tag], v111\n" \
  "s_waitcnt vmcnt(4)\n" \
  "v_cmpx_eq_u32_e32 vcc, %[tag], v113\n" \
  "v_cmpx_eq_u32_e32 vcc, %[tag], v115\n" \
  "s_waitcnt vmcnt(3)\n" \
  "v_cmpx_eq_u32_e32 vcc, %[tag], v117\n" \
  "v_cmpx_eq_u32_e32 vcc, %[tag], v119\n" \
  "s_cmp_eq_u64 vcc, -1\n" \
  "s_mov_b64 exec, -1\n" \
  "s_cbranch_scc1 3f\n" \
  "buffer_load_dwordx4 v[108:111], %[vc], %[rs], 0 offen sc1\n" \
  "buffer_load_dwordx4 v[112:115], %[va], %[rs], 0 offen sc1\n" \
  "buffer_load_dwordx4 v[116:119], %[vb], %[rs], 0 offen sc1\n" \
  "s_add_u32 %[cnt], %[cnt], 1\n" \
  "s_sleep 5\n" \
  "s_cmp_lt_u32 %[cnt], 0x100000\n" \
  "s_cbranch_scc1 1b\n" \
  "s_waitcnt vmcnt(0)\n" \
  "2:\n" \
  "v_mov_b32 %[x0], v96\n" \
  "v_mov_b32 %[z0], v98\n" \
  "v_mov_b32 %[x1], v100\n" \
  "v_mov_b32 %[z1], v102\n" \
  "v_mov_b32 %[x2], v104\n" \
  "v_mov_b32 %[z2], v106\n" \
  "s_branch 9f\n" \
  "3:\n" \
  "v_mov_b32 %[x0], v108\n" \
  "v_mov_b32 %[z0], v110\n" \
  "v_mov_b32 %[x1], v112\n" \
  "v_mov_b32 %[z1], v114\n" \
  "v_mov_b32 %[x2], v116\n" \
  "v_mov_b32 %[z2], v118\n" \
  "9:\n" \
  "s_nop 1\n"

typedef u32 u32x16 __attribute__((ext_vector_type(16)));
template <int N, int D, int SP, class LD>
__device__ __forceinline__ u32 poll(u32x4 (&res)[N], u32 tag, LD&& ld) {
  u32x4 buf[D][N];
#pragma unroll
  for (int d = 0; d < D; ++d) {
    if (d) __builtin_amdgcn_s_sleep(SP);
    MEMCLOB();
#pragma unroll
    for (int k = 0; k < N; ++k) buf[d][k] = ld(k);
    SB();
  }
  u32 spin = 0;
  for (; spin < SPIN_CAP; ++spin) {
    bool done = false;
#pragma unroll
    for (int d = 0; d < D; ++d) {
      bool ok = true;
#pragma unroll
      for (int k = 0; k < N; ++k) { res[k] = buf[d][k]; ok &= (res[k].y == tag) & (res[k].w == tag); }
      if (__all(ok)) { done = true; spin = spin * D + d; break; }
      MEMCLOB();
#pragma unroll
      for (int k = 0; k < N; ++k) buf[d][k] = ld(k);
      SB();
    }
    if (done) break;
  }
  return spin;
}
__global__ void __launch_bounds__(384, 2)
fused_decode(const char* __restrict__ x, const char* __restrict__ Kc, const char* __restrict__ Vc, const char* __restrict__ wq,
             const char* __restrict__ wg, const char* __restrict__ wu, char* __restrict__ ws, const char* __restrict__ wo_,
             const char* __restrict__ wd_, char* __restrict__ out_
             ) {
  __shared__ __attribute__((aligned(16))) u32 s_q[64];
  __shared__ __attribute__((aligned(16))) u32 s_o[128];
  __shared__ __attribute__((aligned(16))) float s_part[4][128];
  __shared__ __attribute__((aligned(16))) float s_ml[8];
  __shared__ __attribute__((aligned(16))) f32x4 s_p2[4][64];
  __shared__ __attribute__((aligned(16))) u32 s_tag[4];

  STAMP_DECL;
  u64 pc0;
  asm volatile("s_getpc_b64 %0" : "=s"(pc0));
  const int lane = threadIdx.x & 63;
  const int wave = __builtin_amdgcn_readfirstlane(threadIdx.x >> 6);
  const int bid = blockIdx.x;
  const int idx = bid >> 3, xcd = bid & 7;
  const int team = xcd * 4 + (idx >> 3), mem = idx & 7;
  const int fgrp = (idx >> 3) * 8 + xcd;
  const int slot = team * 8 + mem;
  const int fj = (mem - 5) * 32 + fgrp;
  const int frg = fj / 5, cblk = mem < 5 ? mem : fj - frg * 5;
  const int g = lane >> 4, n = lane & 15, bsel = (lane >> 3) & 1, m7 = lane & 7;
  const u32 hm = (m7 & 1) ? 0xffff0000u : 0x0000ffffu;
  const u32x4 mask = {(m7 >> 1) == 0 ? hm : 0u, (m7 >> 1) == 1 ? hm : 0u, (m7 >> 1) == 2 ? hm : 0u, (m7 >> 1) == 3 ? hm : 0u};
  const auto rcode = __builtin_amdgcn_make_buffer_rsrc((void*)(pc0 & ~255ull), 0, CODE_BYTES, 0x00020000);
  u32 csink = __builtin_amdgcn_raw_buffer_load_b32(rcode, threadIdx.x * 64, 0, 0);
  asm volatile("" :: "s"(x), "s"(Kc), "s"(Vc), "s"(wq), "s"(wg), "s"(wu), "s"(ws));
  const u32 t0 = now();
  auto late_arg = [&](int i) INL {
    typedef char* const __attribute__((address_space(4))) * kptr;
    kptr kp = (kptr)__builtin_amdgcn_kernarg_segment_ptr();
    asm volatile("" : "+s"(kp));
    return kp[i];
  };
  const auto rws = __builtin_amdgcn_make_buffer_rsrc((void*)ws, 0, WS_END, 0x00020000);

  if (__builtin_expect(wave < 2, 1)) {
    __builtin_amdgcn_s_setprio(3);
    const auto rx = __builtin_amdgcn_make_buffer_rsrc((void*)x, 0, 2560, 0x00020000);
    const bool active = wave == 0 || fgrp < 27;
    const int row0 = (mem >> 1) * 160 + g * 40;
    u32 tag = 0;
    if (active) {
      const u32 e = __builtin_amdgcn_raw_buffer_load_b32(rws, 0, 0, SC1);
      const u32 so = bsel * 1280 + (g * 8 + m7) * 40, oo = bsel * 1280 + row0 * 2;
      u32 xs[10], xo[20];
      { u32x4 a = __builtin_amdgcn_raw_buffer_load_b128(rx, so, 0, 0), b = __builtin_amdgcn_raw_buffer_load_b128(rx, so + 16, 0, 0);
        u32x2 c = __builtin_amdgcn_raw_buffer_load_b64(rx, so + 32, 0, 0);
        xs[0] = a.x; xs[1] = a.y; xs[2] = a.z; xs[3] = a.w; xs[4] = b.x; xs[5] = b.y; xs[6] = b.z; xs[7] = b.w; xs[8] = c.x; xs[9] = c.y; }
#pragma unroll
      for (int i = 0; i < 5; ++i) {
        const u32x4 a = __builtin_amdgcn_raw_buffer_load_b128(rx, oo + i * 16, 0, 0);
        xo[4 * i] = a.x; xo[4 * i + 1] = a.y; xo[4 * i + 2] = a.z; xo[4 * i + 3] = a.w;
      }
      const u32 rstride = wave == 0 ? 8192u : 3456u;
      const char* wp = wave == 0 ? wq + team * 256 + (mem & 1) * 128 : ((mem & 1) ? wu : wg) + fgrp * 128;
      wp += (size_t)(row0 + bsel * 20) * rstride + m7 * 16;
      u32x4 w[20];
#pragma unroll
      for (int i = 0; i < 20; ++i) {
        const gu32x4* p = (const gu32x4*)(wp + (size_t)i * rstride);
        w[i] = __builtin_nontemporal_load(p);
      }
      SB();
      STAMP(6);
      float s = 0.f;
#pragma unroll
      for (int i = 0; i < 10; ++i) s = dot2(xs[i], 0x3f803f80u, s);
      s += dppf<QP_X1>(s); s += dppf<QP_X2>(s); s += dppf<HALF_MIRROR>(s); s = xsum16(s); s = xsum32(s);
      const float mean = s * (1.f / 640.f);
      float v = 0.f;
#pragma unroll
      for (int i = 0; i < 10; ++i) { float a = bf_lo(xs[i]) - mean, b = bf_hi(xs[i]) - mean; v = fmaf(a, a, v); v = fmaf(b, b, v); }
      v += dppf<QP_X1>(v); v += dppf<QP_X2>(v); v += dppf<HALF_MIRROR>(v); v = xsum16(v); v = xsum32(v);
      asm volatile("" :: "v"(csink));
      const float mb = rbf(mean);
      const float den = rbf(__builtin_sqrtf(rbf(rbf(v * (1.f / 640.f)) + 1.0013580322265625e-05f)));
      const float rden = 1.f / den;
      tag = __builtin_amdgcn_readfirstlane(e) + TAG_STEP;
      __builtin_amdgcn_raw_buffer_store_b128((u32x4){t0, tag, t0, tag}, rws, (bid < 8 && wave == 0) ? WS_ST + (lane >> 3) * 1024 + bid * 128 + m7 * 16 : OOB, 0, SC1);
      SB();
      STAMP(1);
      f32x4 acc = {0.f, 0.f, 0.f, 0.f}, accb = {0.f, 0.f, 0.f, 0.f};
#pragma unroll
      for (int i = 0; i < 10; ++i) {
        const u32 d = pk_bf16(bf_lo(xo[i]) - mb, bf_hi(xo[i]) - mb), d2 = pk_bf16(bf_lo(xo[10 + i]) - mb, bf_hi(xo[10 + i]) - mb);
        const float a = bf_lo(d) * rden, b = bf_hi(d) * rden, a2 = bf_lo(d2) * rden, b2 = bf_hi(d2) * rden;
        acc = gemv_step(pk_bf16(a, a), mask, w[2 * i], acc);
        accb = gemv_step(pk_bf16(a2, a2), mask, w[2 * i], accb);
        acc = gemv_step(pk_bf16(b, b), mask, w[2 * i + 1], acc);
        accb = gemv_step(pk_bf16(b2, b2), mask, w[2 * i + 1], accb);
      }
      float p0 = (n < 8 ? acc.x : accb.z) + dppf<ROW_ROR8>(n < 8 ? acc.z : accb.x);
      float p1 = (n < 8 ? acc.y : accb.w) + dppf<ROW_ROR8>(n < 8 ? acc.w : accb.y);
      __builtin_amdgcn_raw_buffer_store_b128((u32x4){__float_as_uint(p0), tag, __float_as_uint(p1), tag}, rws,
                                             (wave == 0 ? WS_GQ : WS_GF) + slot * 1024 + lane * 16, 0, SC1);
      STAMP(2);
    }
    const u32 p2o = (WS_G2 + (max(fj, -1) + 1 + (mem < 5 ? team * 5 + mem : 159)) * 2048 + lane * 16) | (fj < 80 ? 0u : OOB);
    auto publish2 = [&](int b, auto rws) INL {
      const f32x2* sp = (const f32x2*)&s_p2[0][(b * 2 + ((lane >> 1) & 1)) * 16 + (lane >> 2)] + (lane & 1);
      const f32x2 t = (sp[0] + sp[128]) + (sp[256] + sp[384]);
      __builtin_amdgcn_raw_buffer_store_b128((u32x4){__float_as_uint(t.x), tag, __float_as_uint(t.y), tag}, rws, p2o + b * 1024, 0, SC1);
    };
    if (wave == 0) {
      const u32 go = WS_GQ + team * 8192 + (lane >> 5) * 1024 + (mem >> 2) * 512 + (lane & 31) * 16;
      u32x4 r[4];
      u32x2 st = {0u, 0u};
      wait_until(t0 + QDELAY);
      STAMP(8);
      for (u32 spin = 0; spin < SPIN_CAP; ++spin) {
        MEMCLOB();
        bool ok = true;
#pragma unroll
        for (int k = 0; k < 4; ++k) r[k] = __builtin_amdgcn_raw_buffer_load_b128(rws, go, k * 2048, SC1);
        st = __builtin_amdgcn_raw_buffer_load_b64(rws, lane < 8 ? WS_ST + mem * 1024 + m7 * 128 : OOB, 0, SC1);
#pragma unroll
        for (int k = 0; k < 4; ++k) ok &= (r[k].y == tag) & (r[k].w == tag);
        if (__all(ok)) break;
      }
      float q0 = 0.f, q1 = 0.f;
#pragma unroll
      for (int k = 0; k < 4; ++k) { q0 += __uint_as_float(r[k].x); q1 += __uint_as_float(r[k].z); }
      s_q[(g >> 1) * 32 + m7 * 4 + (g & 1) * 2 + bsel] = pk_bf16(q0, q1);
      int dmean = 0;
      int d = (int)(st.x - t0);
      d += dppu<QP_X1>(d); d += dppu<QP_X2>(d); d += dppu<HALF_MIRROR>(d);
      if (__all(st.y == tag || lane >= 8)) dmean = min(max(d >> 3, -200), 200);
      if (lane == 0) *(u32x2*)s_tag = (u32x2){tag, (u32)dmean};
      STAMP(3);
      wg_barrier();
      {
      wg_barrier();
      const f32x4 ml0 = *(const f32x4*)&s_ml[0], ml1 = *(const f32x4*)&s_ml[4];
      const int l32 = lane & 31;
      const f32x4 a0 = *(const f32x4*)&s_part[0][4 * l32], a1 = *(const f32x4*)&s_part[1][4 * l32];
      const f32x4 a2 = *(const f32x4*)&s_part[2][4 * l32], a3 = *(const f32x4*)&s_part[3][4 * l32];
      const float M = fmaxf(fmaxf(ml0.x, ml0.z), fmaxf(ml1.x, ml1.z));
      const float w0 = ex2((ml0.x - M) * L2E), w1 = ex2((ml0.z - M) * L2E), w2 = ex2((ml1.x - M) * L2E), w3 = ex2((ml1.z - M) * L2E);
      const f32x4 o = w0 * a0 + w1 * a1 + w2 * a2 + w3 * a3;
      float L = w0 * ml0.y + w1 * ml0.w + w2 * ml1.y + w3 * ml1.w;
      u32 c0 = pk_bf16(o.x, o.y), c1 = pk_bf16(o.z, o.w);
      asm("" : "+v"(c0), "+v"(c1), "+v"(L));
      __builtin_amdgcn_raw_buffer_store_b128((u32x4){lane < 32 ? c0 : __float_as_uint(M), tag, lane < 32 ? c1 : __float_as_uint(L), tag},
                                             rws, lane < 40 ? WS_G1 + slot * 640 + lane * 16 : OOB, 0, SC1);
      STAMP(4);
      wg_barrier();
      wg_barrier();
      publish2(0, rws);
      publish2(1, rws);
      STAMP(5);
      }
    } else {
      wg_barrier();
      tag = s_tag[0];
      tag = __builtin_amdgcn_readfirstlane(tag);
      const int rid = team * 5 + mem, ob = rid >= 80 ? 1 : 0, ch = rid - ob * 80;
      const u32 oofs = (mem < 5 && lane < 4) ? (u32)(rid * 16 + lane * 4) : OOB;
      char* const outp = late_arg(9);
      const auto rout = __builtin_amdgcn_make_buffer_rsrc((void*)outp, 0, 2560, 0x00020000);
      const u32 xres = __builtin_amdgcn_raw_buffer_load_b32(rx, oofs, 0, 0);
      __builtin_amdgcn_raw_buffer_store_b32(xres, rout, oofs, 0, SC1);
      if (active && mem == HMEM) {
        const u32 go = WS_GF + team * 8192 + lane * 16;
        u32x4 rg[8];
        for (u32 spin = 0; spin < SPIN_CAP; ++spin) {
          MEMCLOB();
          bool ok = true;
#pragma unroll
          for (int k = 0; k < 8; ++k) rg[k] = __builtin_amdgcn_raw_buffer_load_b128(rws, go, k * 1024, SC1);
#pragma unroll
          for (int k = 0; k < 8; ++k) ok &= (rg[k].y == tag) & (rg[k].w == tag);
          if (__all(ok)) break;
        }
        float gg[2] = {0.f, 0.f}, uu[2] = {0.f, 0.f}, hh[2];
#pragma unroll
        for (int k = 0; k < 8; k += 2) {
          gg[0] += __uint_as_float(rg[k].x); gg[1] += __uint_as_float(rg[k].z);
          uu[0] += __uint_as_float(rg[k + 1].x); uu[1] += __uint_as_float(rg[k + 1].z);
        }
#pragma unroll
        for (int k = 0; k < 2; ++k) {
          const float a = rbf(gg[k]);
          const float z = 0.7978845608028654f * (a + 0.044715f * a * a * a);
          const float ge = a / (1.f + ex2(-2.f * L2E * z));
          hh[k] = rbf(ge) * rbf(uu[k]);
        }
        __builtin_amdgcn_raw_buffer_store_b64((u32x2){pk_bf16(hh[0], hh[1]), tag}, rws,
                                              WS_GH + ((g >> 1) * 864 + fgrp * 32 + m7 * 4 + (g & 1) * 2 + bsel) * 8, 0, SC1);
      }
      if (mem >= 5) {
        u32x2 h0 = {0u, 0u}, h1 = {0u, 0u};
        if (fj < 80) {
          const u32 go = lane < 54 ? WS_GH + (frg * 54 + lane) * 8 : OOB;
          wait_until(t0 + HDELAY);
#pragma unroll 1
          for (u32 spin = 0; spin < SPIN_CAP; ++spin) {
            MEMCLOB();
            h0 = __builtin_amdgcn_raw_buffer_load_b64(rws, go, 0, SC1);
            h1 = __builtin_amdgcn_raw_buffer_load_b64(rws, go, 864 * 8, SC1);
            if (__all(lane >= 54 || ((h0.y == tag) & (h1.y == tag)))) break;
            __builtin_amdgcn_s_sleep(16);
          }
        }
        s_o[lane] = h0.x; s_o[64 + lane] = h1.x;
      }
      STAMP(3);
      {
      const u32x4 srd = {(u32)(u64)ws, (u32)((u64)ws >> 32) & 0xffffu, WS_END, 0x00020000u};
      wg_barrier();
      u32x16 ka0, ka1, ka2, ka3, ka4;
      if (mem >= 5) {
        wg_barrier();
        wg_barrier();
      } else {
      {
        const u32 gb = WS_G1 + (team * 8 + (lane >> 5) * 4) * 640;
        const u32 ga = gb + (lane & 31) * 16, gm = gb + (lane & 3) * 640 + 512;
        u32x4 ra[5];
        if (PDELAY) wait_until(now() + PDELAY);
        STAMP(6);
        u32 np = 0;
        asm volatile(POLL1_ASM
                     : [x0] "=&v"(ra[0].x), [z0] "=&v"(ra[0].z), [x1] "=&v"(ra[1].x), [z1] "=&v"(ra[1].z), [x2] "=&v"(ra[2].x), [z2] "=&v"(ra[2].z),
                       [x3] "=&v"(ra[3].x), [z3] "=&v"(ra[3].z), [x4] "=&v"(ra[4].x), [z4] "=&v"(ra[4].z), [cnt] "+s"(np),
                       "=&{v[128:143]}"(ka0), "=&{v[144:159]}"(ka1), "=&{v[160:175]}"(ka2), "=&{v[176:191]}"(ka3), "=&{v[192:207]}"(ka4)
                     : [va] "v"(ga), [vm] "v"(gm), [rs] "s"(srd), [tag] "s"(tag) : "vcc", "scc", "memory");
        const u32x4 rm = ra[4];
        STAMP(7);
        const float mc = __uint_as_float(rm.x);
        float M = fmaxf(mc, dppf<QP_X1>(mc)); M = fmaxf(M, dppf<QP_X2>(M));
        float wc = ex2((mc - M) * L2E);
        float L = wc * __uint_as_float(rm.z);
        L += dppf<QP_X1>(L); L += dppf<QP_X2>(L);
        wc *= __builtin_amdgcn_rcpf(L);
        const float c0 = dppf<0x00>(wc), c1 = dppf<0x55>(wc), c2 = dppf<0xAA>(wc), c3 = dppf<0xFF>(wc);
        const float o0 = c0 * bf_lo(ra[0].x) + c1 * bf_lo(ra[1].x) + c2 * bf_lo(ra[2].x) + c3 * bf_lo(ra[3].x);
        const float o1 = c0 * bf_hi(ra[0].x) + c1 * bf_hi(ra[1].x) + c2 * bf_hi(ra[2].x) + c3 * bf_hi(ra[3].x);
        const float o2 = c0 * bf_lo(ra[0].z) + c1 * bf_lo(ra[1].z) + c2 * bf_lo(ra[2].z) + c3 * bf_lo(ra[3].z);
        const float o3 = c0 * bf_hi(ra[0].z) + c1 * bf_hi(ra[1].z) + c2 * bf_hi(ra[2].z) + c3 * bf_hi(ra[3].z);
        *(u32x2*)&s_o[lane * 2] = (u32x2){pk_bf16(o0, o1), pk_bf16(o2, o3)};
      }
      wg_barrier();
      wg_barrier();
      {
        const u32 go = WS_G2 + ((lane >> 2) * 5 + (ch >> 4)) * 2048 + ob * 1024 + (ch & 15) * 64 + (lane & 3) * 16;
        u32x4 rr[3];
        if (P2DELAY) wait_until(now() + P2DELAY);
        u32 np = 0;
        u32x16 kb0, kb1;
        asm volatile(POLL2_ASM
                     : [x0] "=&v"(rr[0].x), [z0] "=&v"(rr[0].z), [x1] "=&v"(rr[1].x), [z1] "=&v"(rr[1].z), [x2] "=&v"(rr[2].x), [z2] "=&v"(rr[2].z), [cnt] "+s"(np),
                       "=&{v[96:111]}"(kb0), "=&{v[112:127]}"(kb1)
                     : [va] "v"(go), [vb] "v"(go + 80 * 2048), [vc] "v"(go + 160 * 2048), [rs] "s"(srd), [tag] "s"(tag) : "vcc", "scc", "memory");
        const u32x4 r0 = rr[0], r1 = rr[1], r2 = rr[2];
        STAMP(4);
        float v0 = __uint_as_float(r0.x) + __uint_as_float(r1.x) + __uint_as_float(r2.x), v1 = __uint_as_float(r0.z) + __uint_as_float(r1.z) + __uint_as_float(r2.z);
        v0 += dppf<ROW_ROR4>(v0); v1 += dppf<ROW_ROR4>(v1);
        v0 += dppf<ROW_ROR8>(v0); v1 += dppf<ROW_ROR8>(v1);
        v0 = xsum16(v0); v1 = xsum16(v1);
        v0 = xsum32(v0); v1 = xsum32(v1);
        __builtin_amdgcn_raw_buffer_store_b32(pk_bf16(bf_lo(xres) + v0, bf_hi(xres) + v1), rout, oofs, 0, OUT_AUX);
        if (bid == 0) __builtin_amdgcn_raw_buffer_store_b32(tag, rws, lane == 0 ? 0u : OOB, 0, SC1);
        STAMP(5);
        asm volatile(ENDWAIT :: "{v[96:111]}"(kb0), "{v[112:127]}"(kb1));
      }
      asm volatile(ENDWAIT :: "{v[128:143]}"(ka0), "{v[144:159]}"(ka1), "{v[160:175]}"(ka2), "{v[176:191]}"(ka3), "{v[192:207]}"(ka4));
      }
      }
    }
  } else {
    const int sw = wave - 2;
    const char* w2 = late_arg(mem < 5 ? 7 : 8);
    const u32 wrows = mem < 5 ? 128 * 1280 : 108 * 1280;
    asm volatile("" :: "s"(w2));
    const size_t reg = (size_t)((mem >> 2) * 32 + team) << 20;
    const auto rK = __builtin_amdgcn_make_buffer_rsrc((void*)(Kc + reg), 0, 1 << 20, 0x00020000);
    const auto rV = __builtin_amdgcn_make_buffer_rsrc((void*)(Vc + reg), 0, 1 << 20, 0x00020000);
    const u32 vo = lane * 16 + ((mem & 3) * 4 + sw) * 4096;
    int dq = 0;
    const u32 tb = t0 + ((OFF0 + sw * (TG / 4)) >> 4);
    u32x4 rb[2 * NS][4];
    auto issue = [&](int j) INL {
      wait_until(tb + ((j * TG + (j > 15 ? GAP : 0) - (j > JV ? (j - JV) * DV : 0) + (j < 2 * NS ? 0 : j - 2 * NS + 1 < CONV ? j - 2 * NS + 1 : CONV) * dq) >> 4));
#pragma unroll
      for (int t = 0; t < 4; ++t) rb[j % (2 * NS)][t] = __builtin_amdgcn_raw_buffer_load_b128(j < 16 ? rK : rV, vo + t * 1024, (j & 15) * 65536, KV_AUX);
      SB();
    };
    STAMP(6);
#pragma unroll
    for (int j = 0; j < 2 * NS; ++j) {
      issue(j);
      if (j == 1) STAMP(7);
      if (j == 7) STAMP(8);
    }
    STAMP(1);
    asm volatile("" :: "v"(csink));
    wg_barrier();
    STAMP(2);
    const u32x4 q = *(const u32x4*)&s_q[n * 4];
    dq = (int)__builtin_amdgcn_readfirstlane(s_tag[1]) * 16 / CONV;
    f32x4 acc0 = {0.f, 0.f, 0.f, 0.f}, acc1 = {0.f, 0.f, 0.f, 0.f};
    float sc[16];
    u32 P[16];
    auto consumeK = [&](int c) INL {
      const u32x4 (&kb)[4] = rb[c % (2 * NS)];
      float d[4];
#pragma unroll
      for (int t = 0; t < 4; ++t) {
        float a = dot2(kb[t].x, q.x, 0.f); a = dot2(kb[t].y, q.y, a); a = dot2(kb[t].z, q.z, a); a = dot2(kb[t].w, q.w, a);
        d[t] = a + dppf<ROW_ROR8>(a);
      }
      float ab = mergef<0xC>(d[0], d[1]), cd = mergef<0xC>(d[2], d[3]);
      ab += dppf<HALF_MIRROR>(ab); cd += dppf<HALF_MIRROR>(cd);
      float s = mergef<0xA>(ab, cd);
      s += dppf<QP_X1>(s); s += dppf<QP_X2>(s);
      sc[c] = rbf(rbf(s) * 0.08838834764831845f);
      SB();
      if (c == 15) {
        float mx = sc[0];
#pragma unroll
        for (int i = 1; i < 16; ++i) mx = fmaxf(mx, sc[i]);
        mx = fmaxf(mx, dppf<ROW_ROR4>(mx)); mx = fmaxf(mx, dppf<ROW_ROR8>(mx)); mx = xmax16(mx); mx = xmax32(mx);
        float lsum = 0.f;
#pragma unroll
        for (int i = 0; i < 16; ++i) { const float p = ex2((sc[i] - mx) * L2E); lsum += p; P[i] = pk_bf16(p, p); }
        lsum += dppf<ROW_ROR4>(lsum); lsum += dppf<ROW_ROR8>(lsum); lsum = xsum16(lsum); lsum = xsum32(lsum);
        if (lane == 0) *(f32x2*)&s_ml[sw * 2] = (f32x2){mx, lsum};
        SB();
      }
    };
    auto consumeV = [&](int c) INL {
      const u32x4 (&vb)[4] = rb[c % (2 * NS)];
      acc0 = gemv_step(dppu<NEWBCAST + 0>(P[c - 16]), mask, vb[0], acc0);
      acc1 = gemv_step(dppu<NEWBCAST + 8>(P[c - 16]), mask, vb[1], acc1);
      acc0 = gemv_step(dppu<NEWBCAST + 4>(P[c - 16]), mask, vb[2], acc0);
      acc1 = gemv_step(dppu<NEWBCAST + 12>(P[c - 16]), mask, vb[3], acc1);
      if (c < 31) asm volatile("" : "+v"(acc0), "+v"(acc1));
      SB();
    };
    auto consume = [&](int c) INL { if (c >= 16) consumeV(c); else consumeK(c); };
    sfor<2 * NS, 32>([&](auto J) INL {
      sfor<cto(J.v - 1), cto(J.v)>([&](auto C) INL { consume(C.v); });
      issue(J.v);
    });
    STAMP(9);
    sfor<cto(31), 31>([&](auto C) INL { consume(C.v); });
    const u32 wlen = fj < 80 ? wrows : 0u;
    const auto rW = __builtin_amdgcn_make_buffer_rsrc((void*)(w2 + (mem < 5 ? team : frg) * wlen), 0, wlen, 0x00020000);
    u32x4 wb[8];
#define WOFF(i) ((i) * 1280)
    const u32 o2 = (sw * 32 + g * 8) * 1280 + cblk * 256 + n * 16;
    const auto rWa = rW;
    auto issueW = [&](int i) INL { wb[i] = __builtin_amdgcn_raw_buffer_load_b128(rWa, o2 + WOFF(i), 0, W2_AUX); };
    const u32 tl = tb + ((31 * TG + GAP - (31 - JV) * DV + CONV * dq + (3 - sw) * (TG / 4)) >> 4);
    wait_until(tl + (mem < 5 ? WGATE : FLATE == 1 ? -1000 : WGATE2));
#pragma unroll
    for (int i = 0; i < WPRE; ++i) issueW(i);
    wait_until(tl + WG2);
#pragma unroll
    for (int i = WPRE; i < WPRE2; ++i) issueW(i);
    SB();
    consume(31);
    acc0 += acc1;
    if (lane < 32) *(f32x4*)&s_part[sw][n * 8 + g * 4] = acc0;
    SB();
    STAMP(3);
#pragma unroll
    for (int i = FLATE == 2 ? WPREF : WPRE2; i < WPOST; ++i) issueW(i);
    SB();
    wg_barrier();
#pragma unroll
    for (int i = WPOST; i < 8; ++i) issueW(i);
    SB();
    f32x4 acc = {0.f, 0.f, 0.f, 0.f};
    {
    wg_barrier();
    STAMP(4);
    {
      const u32x4 ow = *(const u32x4*)&s_o[bsel * 64 + sw * 16 + g * 4];
      f32x4 accb = {0.f, 0.f, 0.f, 0.f};
#define WO_STEP(i, c) acc = gemv_step(__builtin_amdgcn_perm(c, c, 0x01000100u), mask, wb[i], acc); \
      accb = gemv_step(__builtin_amdgcn_perm(c, c, 0x03020302u), mask, wb[1 + i], accb)
      WO_STEP(0, ow.x); WO_STEP(2, ow.y); WO_STEP(4, ow.z); WO_STEP(6, ow.w);
      acc += accb;
    }
    s_p2[sw][lane] = acc;
    STAMP(5);
    }
    wg_barrier();
  }
  STAMP_DUMP();
}

extern "C" void kernel_launch(void* const* d_in, const int* in_sizes, int n_in, void* d_out, int out_size, void* d_ws, size_t ws_size,
                              hipStream_t stream) {
  (void)in_sizes; (void)n_in; (void)out_size; (void)ws_size;
  fused_decode<<<256, 384, 0, stream>>>((const char*)d_in[0], (const char*)d_in[1], (const char*)d_in[2], (const char*)d_in[3],
                                        (const char*)d_in[6], (const char*)d_in[7], (char*)d_ws, (const char*)d_in[4],
                                        (const char*)d_in[8], (char*)d_out
                                        );
}
```
